```python
import torch

def _softmax(x, axis=-1):
    return torch.softmax(x, dim=axis)

def _gelu(x):
    return torch.nn.functional.gelu(x, approximate='tanh')

def _silu(x):
    return torch.nn.functional.silu(x)

def _sigmoid(x):
    return torch.sigmoid(x)

def _layernorm(x, axis=-1, eps=1e-05):
    m = x.mean(dim=axis, keepdim=True)
    v = x.var(dim=axis, keepdim=True, unbiased=False)
    return (x - m) / torch.sqrt(v + eps)

def _scatter_add(tgt, idx, src, axis=0):
    return tgt.clone().index_add_(axis, idx, src)

def _segment_sum(src, seg, n):
    out = torch.zeros((n,) + src.shape[1:], dtype=src.dtype)
    return out.index_add_(0, seg, src)

def _pad(x, pw):
    flat = tuple((v for pair in reversed(pw) for v in pair))
    return torch.nn.functional.pad(x, flat)
kernel_name = 'adaLN_cond__mha_block_cached__geglu_ffn__decode'

def setup_inputs():

    _w = lambda a: (lambda t: t.to(torch.bfloat16) if t.is_floating_point() else t)(torch.as_tensor(a))

    def _A_setup():
        B, Sq, D = (4, 1, 512)
        H, Sk, Dh = (32, 4096, 128)
        return {'x': _w((lambda *s: torch.randn(*s, dtype=torch.bfloat16) * 0.1)(B, Sq, D)), 'k_cache': _w((lambda *s: torch.randn(*s, dtype=torch.bfloat16) * 0.8)(B, H, Sk, Dh)), 'v_cache': _w((lambda *s: torch.randn(*s, dtype=torch.bfloat16) * 0.8)(B, H, Sk, Dh)), 'wq': _w((lambda *s: torch.randn(*s, dtype=torch.bfloat16) * 0.8)(D, H * Dh) * D ** (-0.5)), 'wo': _w((lambda *s: torch.randn(*s, dtype=torch.bfloat16) * 0.8)(H * Dh, D) * (H * Dh) ** (-0.5))}
    _A_in = _A_setup()

    def _B_setup():
        B, S, D, F = (4, 1, 512, 1792)
        return {'x': _w((lambda *s: torch.randn(*s, dtype=torch.bfloat16) * 0.1)(B, S, D)), 'w_gate': _w((lambda *s: torch.randn(*s, dtype=torch.bfloat16) * 0.1)(D, F)), 'w_up': _w((lambda *s: torch.randn(*s, dtype=torch.bfloat16) * 0.1)(D, F)), 'w_down': _w((lambda *s: torch.randn(*s, dtype=torch.bfloat16) * 0.1)(F, D))}
    _B_in = _B_setup()
    return {'A_x': _A_in['x'], 'A_k_cache': _A_in['k_cache'], 'A_v_cache': _A_in['v_cache'], 'A_wq': _A_in['wq'], 'A_wo': _A_in['wo'], 'B_x': _B_in['x'], 'B_w_gate': _B_in['w_gate'], 'B_w_up': _B_in['w_up'], 'B_w_down': _B_in['w_down'], 'c': _w((lambda *s: torch.randn(*s, dtype=torch.bfloat16) * 0.8)(4, 512) * 8.0), 'w_mod': _w((lambda *s: torch.randn(*s, dtype=torch.bfloat16) * 0.8)(512, 6 * 512) * 512 ** (-0.5) * 8.0)}

def reference(A_x, A_k_cache, A_v_cache, A_wq, A_wo, B_x, B_w_gate, B_w_up, B_w_down, c, w_mod):

    def _A_ref(x, k_cache, v_cache, wq, wo):
        B, Sq, D = x.shape
        H, Dh = (32, 128)
        q = (lambda x, *d: x.permute(*d))((x @ wq).reshape(B, Sq, H, Dh), 0, 2, 1, 3)
        scores = torch.einsum('bhqd,bhkd->bhqk', q, k_cache) * Dh ** (-0.5)
        p = _softmax(scores, axis=-1)
        o = torch.einsum('bhqk,bhkd->bhqd', p, v_cache)
        return (lambda x, *d: x.permute(*d))(o, 0, 2, 1, 3).reshape(B, Sq, H * Dh) @ wo

    def _B_ref(x, w_gate, w_up, w_down):
        return _gelu(x @ w_gate) * (x @ w_up) @ w_down
    x = A_x
    mod = (_silu(c) @ w_mod).reshape(c.shape[0], 6, 1, -1)
    sa, ca, ga, sb, cb, gb = (mod[:, 0].clone(), mod[:, 1].clone(), mod[:, 2].clone(), mod[:, 3].clone(), mod[:, 4].clone(), mod[:, 5].clone())
    h = _layernorm(x, axis=-1) * (1 + ca) + sa
    x = x + ga * _A_ref(h, A_k_cache, A_v_cache, A_wq, A_wo)
    h = _layernorm(x, axis=-1) * (1 + cb) + sb
    x = x + gb * _B_ref(h, B_w_gate, B_w_up, B_w_down)
    return x
```

```cpp
#include <hip/hip_runtime.h>
#include <hip/hip_bf16.h>
#include <cstdint>

typedef unsigned short u16;
typedef __attribute__((ext_vector_type(4))) unsigned u32x4;
typedef __attribute__((ext_vector_type(2))) __bf16 bf16x2;

#define NB 4
#define DM 512
#define NH 32
#define SK 4096
#define DH 128
#define FF 1792
#define NSPLIT 2
#define KPS (SK / NSPLIT)

__device__ __forceinline__ float bf2f(u16 u) { return __uint_as_float(((unsigned)u) << 16); }
__device__ __forceinline__ u16 f2bf(float f) { unsigned u = __float_as_uint(f); u += 0x7fffu + ((u >> 16) & 1u); return (u16)(u >> 16); }
__device__ __forceinline__ float rbf(float f) { return bf2f(f2bf(f)); }
__device__ __forceinline__ float lo16(unsigned d) { return __uint_as_float(d << 16); }
__device__ __forceinline__ float hi16(unsigned d) { return __uint_as_float(d & 0xffff0000u); }
__device__ __forceinline__ float dot2(unsigned k, unsigned q, float acc) {
    return __builtin_amdgcn_fdot2_f32_bf16(__builtin_bit_cast(bf16x2, k), __builtin_bit_cast(bf16x2, q), acc, false);
}
template <int CTRL> __device__ __forceinline__ float dpp(float x) {
    return __builtin_bit_cast(float, __builtin_amdgcn_mov_dpp(__builtin_bit_cast(int, x), CTRL, 0xf, 0xf, true));
}
constexpr int XOR1 = 0xB1, XOR2 = 0x4E, XOR7 = 0x141, XOR8 = 0x128;

__device__ void gemv4_block(const float* __restrict__ Alds, int lda, const u16* __restrict__ W, int N, int col0, int k0, int k1,
                            float* __restrict__ red  , float* __restrict__ res  ) {
    const int t = threadIdx.x, cg = t & 7, kg = t >> 3;
    float acc[4][8];
#pragma unroll
    for (int b = 0; b < 4; ++b)
#pragma unroll
        for (int j = 0; j < 8; ++j) acc[b][j] = 0.f;
    for (int k = k0 + kg; k < k1; k += 32) {
        const u32x4 w = *reinterpret_cast<const u32x4*>(W + (size_t)k * N + col0 + cg * 8);
        float wf[8];
#pragma unroll
        for (int j = 0; j < 4; ++j) { wf[2 * j] = lo16(w[j]); wf[2 * j + 1] = hi16(w[j]); }
#pragma unroll
        for (int b = 0; b < 4; ++b) { const float a = Alds[b * lda + k];
#pragma unroll
            for (int j = 0; j < 8; ++j) acc[b][j] = fmaf(a, wf[j], acc[b][j]); }
    }
#pragma unroll
    for (int b = 0; b < 4; ++b)
#pragma unroll
        for (int j = 0; j < 8; ++j) red[(kg * 4 + b) * 64 + cg * 8 + j] = acc[b][j];
    __syncthreads();
    { const int b = t >> 6, col = t & 63; float s = 0.f;
      for (int g = 0; g < 32; ++g) s += red[(g * 4 + b) * 64 + col];
      res[b * 64 + col] = s; }
    __syncthreads();
}

__global__ void __launch_bounds__(256) k_mod(const u16* __restrict__ c, const u16* __restrict__ wmod, float* __restrict__ mod) {
    __shared__ float A[4 * DM]; __shared__ float red[32 * 4 * 64]; __shared__ float res[256];
    for (int i = threadIdx.x; i < 4 * DM; i += 256) { float v = bf2f(c[i]); A[i] = rbf(v / (1.f + __expf(-v))); }
    __syncthreads();
    const int col0 = blockIdx.x * 64;
    gemv4_block(A, DM, wmod, 6 * DM, col0, 0, DM, red, res);
    { const int b = threadIdx.x >> 6, col = threadIdx.x & 63; mod[b * (6 * DM) + col0 + col] = rbf(res[threadIdx.x]); }
}

__device__ void ln_mod_block(const float* __restrict__ xin, const float* __restrict__ mod, int off_shift, int off_scale, float* __restrict__ hout) {
    const int b = threadIdx.x >> 6, lane = threadIdx.x & 63;
    float v[8]; float s = 0.f;
#pragma unroll
    for (int j = 0; j < 8; ++j) { v[j] = xin[b * DM + lane * 8 + j]; s += v[j]; }
#pragma unroll
    for (int o = 32; o > 0; o >>= 1) s += __shfl_xor(s, o);
    const float mean = s * (1.f / DM);
    float q = 0.f;
#pragma unroll
    for (int j = 0; j < 8; ++j) { float d = v[j] - mean; q += d * d; }
#pragma unroll
    for (int o = 32; o > 0; o >>= 1) q += __shfl_xor(q, o);
    const float var = rbf(q * (1.f / DM));
    const float mb = rbf(mean);
    const float den = rbf(sqrtf(rbf(var + 1e-5f)));
#pragma unroll
    for (int j = 0; j < 8; ++j) {
        const int i = lane * 8 + j;
        const float xn = rbf(rbf(v[j] - mb) / den);
        const float sc = rbf(1.f + mod[b * (6 * DM) + off_scale + i]);
        hout[b * DM + i] = rbf(rbf(xn * sc) + mod[b * (6 * DM) + off_shift + i]);
    }
}

__global__ void __launch_bounds__(256) k_q(const u16* __restrict__ x, const float* __restrict__ mod, const u16* __restrict__ wq, float* __restrict__ q) {
    __shared__ float X[4 * DM]; __shared__ float A[4 * DM]; __shared__ float red[32 * 4 * 64]; __shared__ float res[256];
    for (int i = threadIdx.x; i < 4 * DM; i += 256) X[i] = bf2f(x[i]);
    __syncthreads();
    ln_mod_block(X, mod, 0, DM, A);
    __syncthreads();
    const int col0 = blockIdx.x * 64;
    gemv4_block(A, DM, wq, NH * DH, col0, 0, DM, red, res);
    { const int b = threadIdx.x >> 6, col = threadIdx.x & 63; q[b * (NH * DH) + col0 + col] = rbf(res[threadIdx.x]); }
}

#define NW 8
__global__ void __launch_bounds__(512) k_attn(const float* __restrict__ q, const u16* __restrict__ kc, const u16* __restrict__ vc, float* __restrict__ part) {
    __shared__ float S[KPS];
    __shared__ float wmax[NW]; __shared__ float wsum[NW];
    __shared__ float ored[NW][DH];
    const int bh = blockIdx.x / NSPLIT, sp = blockIdx.x % NSPLIT;
    const int b = bh / NH, h = bh % NH;
    const int tid = threadIdx.x, lane = tid & 63, wave = __builtin_amdgcn_readfirstlane(tid >> 6), r = lane >> 4, c = lane & 15;
    constexpr int KPW = KPS / NW;
    constexpr int NIT = KPW / 16;
    u32x4 qp;
    { const float* qs = q + b * (NH * DH) + h * DH + 8 * c;
#pragma unroll
      for (int j = 0; j < 4; ++j) qp[j] = (unsigned)f2bf(qs[2 * j]) | ((unsigned)f2bf(qs[2 * j + 1]) << 16); }
    const size_t base = ((size_t)bh * SK + (size_t)sp * KPS + (size_t)wave * KPW) * DH;
    const u16* kp = kc + base + (size_t)r * DH + 8 * c;
    const u16* vp = vc + base + (size_t)r * DH + 8 * c;
    u32x4 kb[2][4];
#pragma unroll
    for (int t = 0; t < 4; ++t) kb[0][t] = __builtin_nontemporal_load(reinterpret_cast<const u32x4*>(kp + (size_t)(0 * 16 + 4 * t) * DH));
#pragma unroll
    for (int t = 0; t < 4; ++t) kb[1][t] = __builtin_nontemporal_load(reinterpret_cast<const u32x4*>(kp + (size_t)(1 * 16 + 4 * t) * DH));
    float lmax = -INFINITY;
#pragma unroll 2
    for (int it = 0; it < NIT; ++it) {
        const int cur = it & 1;
        float x[4];
#pragma unroll
        for (int t = 0; t < 4; ++t) { float d = 0.f;
#pragma unroll
            for (int j = 0; j < 4; ++j) d = dot2(kb[cur][t][j], qp[j], d);
            x[t] = d; }
        if (it + 2 < NIT) {
#pragma unroll
            for (int t = 0; t < 4; ++t) kb[cur][t] = __builtin_nontemporal_load(reinterpret_cast<const u32x4*>(kp + (size_t)((it + 2) * 16 + 4 * t) * DH));
        }
        float y0 = ((c & 8) ? x[2] : x[0]) + dpp<XOR8>((c & 8) ? x[0] : x[2]);
        float y1 = ((c & 8) ? x[3] : x[1]) + dpp<XOR8>((c & 8) ? x[1] : x[3]);
        float z = ((c & 4) ? y1 : y0) + dpp<XOR7>((c & 4) ? y0 : y1);
        z += dpp<XOR2>(z); z += dpp<XOR1>(z);
        const int ti = ((c >> 3) & 1) * 2 + ((c >> 2) & 1);
        const float sc = rbf(rbf(z) * 0.08838834764831845f);
        lmax = fmaxf(lmax, sc);
        if ((c & 3) == 0) S[wave * KPW + it * 16 + r * 4 + ti] = sc;
    }
#pragma unroll
    for (int o = 32; o > 0; o >>= 1) lmax = fmaxf(lmax, __shfl_xor(lmax, o));
    if (lane == 0) wmax[wave] = lmax;
    u32x4 vb[2][4];
#pragma unroll
    for (int t = 0; t < 4; ++t) vb[0][t] = __builtin_nontemporal_load(reinterpret_cast<const u32x4*>(vp + (size_t)(0 * 16 + 4 * t) * DH));
#pragma unroll
    for (int t = 0; t < 4; ++t) vb[1][t] = __builtin_nontemporal_load(reinterpret_cast<const u32x4*>(vp + (size_t)(1 * 16 + 4 * t) * DH));
    __syncthreads();
    float M = wmax[0];
#pragma unroll
    for (int w = 1; w < NW; ++w) M = fmaxf(M, wmax[w]);
    float ls = 0.f;
    { float4* sp4 = reinterpret_cast<float4*>(S + wave * KPW) + lane;
      float4 v4 = *sp4;
      v4.x = __expf(v4.x - M); v4.y = __expf(v4.y - M); v4.z = __expf(v4.z - M); v4.w = __expf(v4.w - M);
      ls = v4.x + v4.y + v4.z + v4.w;
      *sp4 = v4; }
#pragma unroll
    for (int o = 32; o > 0; o >>= 1) ls += __shfl_xor(ls, o);
    if (lane == 0) wsum[wave] = ls;
    float acc[8];
#pragma unroll
    for (int j = 0; j < 8; ++j) acc[j] = 0.f;
#pragma unroll 2
    for (int it = 0; it < NIT; ++it) {
        const int cur = it & 1;
        const float4 p4 = *reinterpret_cast<const float4*>(S + wave * KPW + it * 16 + r * 4);
        const float pp[4] = {p4.x, p4.y, p4.z, p4.w};
#pragma unroll
        for (int t = 0; t < 4; ++t) {
#pragma unroll
            for (int j = 0; j < 4; ++j) { acc[2 * j] = fmaf(pp[t], lo16(vb[cur][t][j]), acc[2 * j]); acc[2 * j + 1] = fmaf(pp[t], hi16(vb[cur][t][j]), acc[2 * j + 1]); }
        }
        if (it + 2 < NIT) {
#pragma unroll
            for (int t = 0; t < 4; ++t) vb[cur][t] = __builtin_nontemporal_load(reinterpret_cast<const u32x4*>(vp + (size_t)((it + 2) * 16 + 4 * t) * DH));
        }
    }
#pragma unroll
    for (int j = 0; j < 8; ++j) { acc[j] += __shfl_xor(acc[j], 16); acc[j] += __shfl_xor(acc[j], 32); }
    if (r == 0) {
#pragma unroll
        for (int j = 0; j < 8; ++j) ored[wave][8 * c + j] = acc[j];
    }
    __syncthreads();
    float* out = part + (size_t)blockIdx.x * (2 + DH);
    if (tid < DH) { float s = 0.f;
#pragma unroll
        for (int w = 0; w < NW; ++w) s += ored[w][tid];
        out[2 + tid] = s; }
    if (tid == 0) { float L = 0.f;
#pragma unroll
        for (int w = 0; w < NW; ++w) L += wsum[w];
        out[0] = M; out[1] = L; }
}

__global__ void __launch_bounds__(256) k_wo(const float* __restrict__ part, const u16* __restrict__ wo, float* __restrict__ ap) {
    __shared__ float A[4 * 512]; __shared__ float red[32 * 4 * 64]; __shared__ float res[256];
    const int ks = blockIdx.y;
    for (int i = threadIdx.x; i < 4 * 512; i += 256) {
        const int b = i >> 9, kk = i & 511, h = ks * 4 + (kk >> 7), d = kk & 127;
        const float* p0 = part + ((size_t)(b * NH + h) * NSPLIT) * (2 + DH);
        float M = -INFINITY;
        for (int s = 0; s < NSPLIT; ++s) M = fmaxf(M, p0[s * (2 + DH)]);
        float L = 0.f, o = 0.f;
        for (int s = 0; s < NSPLIT; ++s) { const float w = __expf(p0[s * (2 + DH)] - M); L += w * p0[s * (2 + DH) + 1]; o += w * p0[s * (2 + DH) + 2 + d]; }
        A[i] = rbf(o / L);
    }
    __syncthreads();
    const int col0 = blockIdx.x * 64;
    gemv4_block(A - ks * 0, 512, wo + (size_t)ks * 512 * DM, DM, col0, 0, 512, red, res);
    { const int b = threadIdx.x >> 6, col = threadIdx.x & 63; ap[(ks * 4 + b) * DM + col0 + col] = res[threadIdx.x]; }
}

__global__ void __launch_bounds__(256) k_gu(const u16* __restrict__ x, const float* __restrict__ ap, const float* __restrict__ mod,
                                            const u16* __restrict__ wg, const u16* __restrict__ wu, float* __restrict__ act, float* __restrict__ x1out) {
    __shared__ float X[4 * DM]; __shared__ float A[4 * DM]; __shared__ float red[32 * 4 * 64]; __shared__ float res[256]; __shared__ float resg[256];
    for (int i = threadIdx.x; i < 4 * DM; i += 256) {
        const int b = i >> 9, j = i & 511;
        float s = 0.f;
        for (int ks = 0; ks < 8; ++ks) s += ap[(ks * 4 + b) * DM + j];
        const float attn = rbf(s);
        const float x1 = rbf(bf2f(x[i]) + rbf(mod[b * (6 * DM) + 2 * DM + j] * attn));
        X[i] = x1;
        if (blockIdx.x == 0) x1out[i] = x1;
    }
    __syncthreads();
    ln_mod_block(X, mod, 3 * DM, 4 * DM, A);
    __syncthreads();
    const int col0 = blockIdx.x * 64;
    gemv4_block(A, DM, wg, FF, col0, 0, DM, red, resg);
    gemv4_block(A, DM, wu, FF, col0, 0, DM, red, res);
    { const int b = threadIdx.x >> 6, col = threadIdx.x & 63;
      const float g = rbf(resg[threadIdx.x]), u = rbf(res[threadIdx.x]);
      const float inner = 0.7978845608028654f * (g + 0.044715f * g * g * g);
      const float gl = rbf(0.5f * g * (1.f + tanhf(inner)));
      act[b * FF + col0 + col] = rbf(gl * u); }
}

__global__ void __launch_bounds__(256) k_down(const float* __restrict__ act, const u16* __restrict__ wd, float* __restrict__ dp) {
    __shared__ float A[4 * 256]; __shared__ float red[32 * 4 * 64]; __shared__ float res[256];
    const int ks = blockIdx.y;
    for (int i = threadIdx.x; i < 4 * 256; i += 256) { const int b = i >> 8, kk = i & 255; A[i] = act[b * FF + ks * 256 + kk]; }
    __syncthreads();
    const int col0 = blockIdx.x * 64;
    gemv4_block(A, 256, wd + (size_t)ks * 256 * DM, DM, col0, 0, 256, red, res);
    { const int b = threadIdx.x >> 6, col = threadIdx.x & 63; dp[(ks * 4 + b) * DM + col0 + col] = res[threadIdx.x]; }
}

__global__ void __launch_bounds__(256) k_fin(const float* __restrict__ dp, const float* __restrict__ x1, const float* __restrict__ mod, u16* __restrict__ out) {
    const int i = blockIdx.x * 256 + threadIdx.x;
    const int b = i >> 9, j = i & 511;
    float s = 0.f;
    for (int ks = 0; ks < 7; ++ks) s += dp[(ks * 4 + b) * DM + j];
    const float ffn = rbf(s);
    out[i] = f2bf(x1[i] + rbf(mod[b * (6 * DM) + 5 * DM + j] * ffn));
}

extern "C" void kernel_launch(void* const* d_in, const int* in_sizes, int n_in, void* d_out, int out_size, void* d_ws, size_t ws_size, hipStream_t stream) {
    const u16* x = (const u16*)d_in[0];
    const u16* kc = (const u16*)d_in[1];
    const u16* vc = (const u16*)d_in[2];
    const u16* wq = (const u16*)d_in[3];
    const u16* wo = (const u16*)d_in[4];
    const u16* wg = (const u16*)d_in[6];
    const u16* wu = (const u16*)d_in[7];
    const u16* wd = (const u16*)d_in[8];
    const u16* c = (const u16*)d_in[9];
    const u16* wmod = (const u16*)d_in[10];
    float* ws = (float*)d_ws;
    float* mod = ws;
    float* q = mod + 4 * 3072;
    float* part = q + 4 * 4096;
    float* ap = part + 128 * NSPLIT * 130;
    float* act = ap + 8 * 4 * 512;
    float* x1 = act + 4 * FF;
    float* dp = x1 + 4 * 512;
    k_mod<<<48, 256, 0, stream>>>(c, wmod, mod);
    k_q<<<64, 256, 0, stream>>>(x, mod, wq, q);
    k_attn<<<128 * NSPLIT, 512, 0, stream>>>(q, kc, vc, part);
    k_wo<<<dim3(8, 8), 256, 0, stream>>>(part, wo, ap);
    k_gu<<<28, 256, 0, stream>>>(x, ap, mod, wg, wu, act, x1);
    k_down<<<dim3(8, 7), 256, 0, stream>>>(act, wd, dp);
    k_fin<<<8, 256, 0, stream>>>(dp, x1, mod, (u16*)d_out);
}
```

```cpp
#include <hip/hip_runtime.h>
#include <hip/hip_bf16.h>
#include <cstdint>

typedef unsigned short u16;
typedef unsigned long long u64;
typedef __attribute__((ext_vector_type(4))) unsigned u32x4;
typedef __attribute__((ext_vector_type(2))) __bf16 bf16x2;

#define NB 4
#define DM 512
#define NH 32
#define SK 4096
#define DH 128
#define FF 1792
#define KPS 2048
#define NW 8
#define KPW (KPS / NW)
#define NIT (KPW / 16)
#define DEPTH 4

#define WS_MOD 0
#define WS_GY 65536
#define WS_GML (WS_GY + 4 * 64 * 64 * 8 * 8)
#define WS_GX1 (WS_GML + 4 * 64 * 2 * 8)
#define WS_GACT (WS_GX1 + 4 * 256 * 8)
#define WS_TMO (WS_GACT + 4 * 896 * 8)
#define WS_END (WS_TMO + 64)
#define TAG_Y 0x71A50001u
#define TAG_X1 0x71A50002u
#define TAG_ACT 0x71A50003u
#define SPIN_CAP 400000

__device__ __forceinline__ float bf2f(u16 u) { return __uint_as_float(((unsigned)u) << 16); }
__device__ __forceinline__ unsigned f2bfu(float f) { unsigned u = __float_as_uint(f); u += 0x7fffu + ((u >> 16) & 1u); return u >> 16; }
__device__ __forceinline__ float rbf(float f) { return __uint_as_float(f2bfu(f) << 16); }
__device__ __forceinline__ float lo16(unsigned d) { return __uint_as_float(d << 16); }
__device__ __forceinline__ float hi16(unsigned d) { return __uint_as_float(d & 0xffff0000u); }
__device__ __forceinline__ float dot2(unsigned k, unsigned q, float acc) {
    return __builtin_amdgcn_fdot2_f32_bf16(__builtin_bit_cast(bf16x2, k), __builtin_bit_cast(bf16x2, q), acc, false);
}
template <int CTRL> __device__ __forceinline__ float dpp(float x) {
    return __builtin_bit_cast(float, __builtin_amdgcn_mov_dpp(__builtin_bit_cast(int, x), CTRL, 0xf, 0xf, true));
}
constexpr int XOR1 = 0xB1, XOR2 = 0x4E, XOR7 = 0x141, XOR8 = 0x128;
__device__ __forceinline__ float swap_add32(float a, float b) {
    auto r = __builtin_amdgcn_permlane32_swap(__float_as_uint(a), __float_as_uint(b), false, false);
    return __uint_as_float(r[0]) + __uint_as_float(r[1]);
}
__device__ __forceinline__ float swap_add16(float a, float b) {
    auto r = __builtin_amdgcn_permlane16_swap(__float_as_uint(a), __float_as_uint(b), false, false);
    return __uint_as_float(r[0]) + __uint_as_float(r[1]);
}
__device__ __forceinline__ u32x4 ldnt(const u16* p) { return __builtin_nontemporal_load(reinterpret_cast<const u32x4*>(p)); }
__device__ __forceinline__ u32x4 ld16(const u16* p) { return *reinterpret_cast<const u32x4*>(p); }
__device__ __forceinline__ void st_gran(u64* p, unsigned tag, unsigned v) { __hip_atomic_store(p, ((u64)tag << 32) | (u64)v, __ATOMIC_RELAXED, __HIP_MEMORY_SCOPE_AGENT); }
__device__ __forceinline__ u64 ld_gran(const u64* p) { return __hip_atomic_load(p, __ATOMIC_RELAXED, __HIP_MEMORY_SCOPE_AGENT); }

__device__ __forceinline__ float treduce32(float (&v)[32], int lane) {
#pragma unroll
    for (int i = 0; i < 32; ++i) v[i] = swap_add32(v[i], v[i]);
#pragma unroll
    for (int i = 0; i < 16; ++i) v[i] = swap_add16(v[i], v[i + 16]);
#pragma unroll
    for (int i = 0; i < 8; ++i) { const bool up = lane & 8; const float keep = up ? v[i + 8] : v[i], send = up ? v[i] : v[i + 8]; v[i] = keep + dpp<XOR8>(send); }
#pragma unroll
    for (int i = 0; i < 4; ++i) { const bool up = lane & 4; const float keep = up ? v[i + 4] : v[i], send = up ? v[i] : v[i + 4]; v[i] = keep + dpp<XOR7>(send); }
#pragma unroll
    for (int i = 0; i < 2; ++i) { const bool up = lane & 2; const float keep = up ? v[i + 2] : v[i], send = up ? v[i] : v[i + 2]; v[i] = keep + dpp<XOR2>(send); }
    { const bool up = lane & 1; const float keep = up ? v[1] : v[0], send = up ? v[0] : v[1]; v[0] = keep + dpp<XOR1>(send); }
    return v[0];
}

struct Params { const u16 *x, *kc, *vc, *wq, *wo, *wg, *wu, *wd, *c, *wmod; u16* out; unsigned char* ws; };

__global__ void __launch_bounds__(512) k_mod(Params p) {
    __shared__ float red[NW * 32];
    const int tid = threadIdx.x, lane = tid & 63, wave = tid >> 6;
    const int sl = (blockIdx.x & 7) * 48 + (blockIdx.x >> 3);
    const u32x4 w = ld16(p.wmod + (size_t)tid * (6 * DM) + sl * 8);
    float a[4];
#pragma unroll
    for (int b = 0; b < 4; ++b) { const float v = bf2f(p.c[b * DM + tid]); a[b] = rbf(v / (1.f + __expf(-v))); }
    { u32x4* z = reinterpret_cast<u32x4*>(p.ws + WS_GY); const int n = (WS_END - WS_GY) / 16;
      for (int i = blockIdx.x * 512 + tid; i < n; i += gridDim.x * 512) z[i] = u32x4{0u, 0u, 0u, 0u}; }
    float v[32];
#pragma unroll
    for (int b = 0; b < 4; ++b)
#pragma unroll
        for (int j = 0; j < 4; ++j) { v[b * 8 + 2 * j] = a[b] * lo16(w[j]); v[b * 8 + 2 * j + 1] = a[b] * hi16(w[j]); }
    const float t = treduce32(v, lane);
    if (lane < 32) red[wave * 32 + lane] = t;
    __syncthreads();
    if (tid < 32) { float s = 0.f;
#pragma unroll
        for (int w8 = 0; w8 < NW; ++w8) s += red[w8 * 32 + tid];
        reinterpret_cast<float*>(p.ws + WS_MOD)[(tid >> 3) * (6 * DM) + sl * 8 + (tid & 7)] = rbf(s); }
}

__global__ void __launch_bounds__(512, 2) k_fused(Params p) {
    __shared__ __attribute__((aligned(16))) float lds[4096 + 2048 + 7168 + 1024];
    float* HB = lds;
    float* QRED = lds + 2048;
    float* S = lds + 4096;
    float* YRED = lds + 6144;
    float* SM = lds + 6144 + 7168;
    float* wmax = SM, *wsum = SM + 8, *obuf = SM + 16, *ored = SM + 144, *red = SM + 160;
    const float* MOD = reinterpret_cast<const float*>(p.ws + WS_MOD);
    const int bid = blockIdx.x, xq = bid & 7, jq = bid >> 3;
    const int h = xq * 4 + (jq >> 3), b = (jq & 7) >> 1, sp = jq & 1;
    const int tid = threadIdx.x, lane = tid & 63, wave = __builtin_amdgcn_readfirstlane(tid >> 6), r = lane >> 4, c = lane & 15;

    u32x4 xr = {0, 0, 0, 0}; float sa[8], ca[8];
    if (wave == 0) {
        xr = ld16(p.x + b * DM + lane * 8);
#pragma unroll
        for (int j = 0; j < 8; ++j) { sa[j] = MOD[b * (6 * DM) + lane * 8 + j]; ca[j] = MOD[b * (6 * DM) + DM + lane * 8 + j]; }
    }
    u32x4 wqr[16];
    { const u16* wp = p.wq + (size_t)(wave * 64 + r) * (NH * DH) + h * DH + 8 * c;
#pragma unroll
      for (int i = 0; i < 16; ++i) wqr[i] = ld16(wp + (size_t)(4 * i) * (NH * DH)); }
    const size_t kvbase = ((size_t)(b * NH + h) * SK + (size_t)sp * KPS + (size_t)wave * KPW) * DH;
    const u16* kp = p.kc + kvbase + (size_t)r * DH + 8 * c;
    const u16* vp = p.vc + kvbase + (size_t)r * DH + 8 * c;
    u32x4 kb[DEPTH][4];
#pragma unroll
    for (int d = 0; d < DEPTH; ++d)
#pragma unroll
        for (int t = 0; t < 4; ++t) kb[d][t] = ldnt(kp + (size_t)(d * 16 + 4 * t) * DH);

    if (wave == 0) {
        float v[8]; float s = 0.f;
#pragma unroll
        for (int j = 0; j < 4; ++j) { v[2 * j] = lo16(xr[j]); v[2 * j + 1] = hi16(xr[j]); }
#pragma unroll
        for (int j = 0; j < 8; ++j) s += v[j];
#pragma unroll
        for (int o = 32; o > 0; o >>= 1) s += __shfl_xor(s, o);
        const float mean = s * (1.f / DM);
        float q2 = 0.f;
#pragma unroll
        for (int j = 0; j < 8; ++j) { const float d = v[j] - mean; q2 += d * d; }
#pragma unroll
        for (int o = 32; o > 0; o >>= 1) q2 += __shfl_xor(q2, o);
        const float var = rbf(q2 * (1.f / DM)), mb = rbf(mean);
        const float den = rbf(sqrtf(rbf(var + 1e-5f)));
#pragma unroll
        for (int j = 0; j < 8; ++j) HB[lane * 8 + j] = rbf(rbf(rbf(rbf(v[j] - mb) / den) * rbf(1.f + ca[j])) + sa[j]);
    }
    __syncthreads();
    {
        float qa[8];
#pragma unroll
        for (int j = 0; j < 8; ++j) qa[j] = 0.f;
#pragma unroll
        for (int i = 0; i < 16; ++i) { const float hv = HB[wave * 64 + 4 * i + r];
#pragma unroll
            for (int j = 0; j < 4; ++j) { qa[2 * j] = fmaf(hv, lo16(wqr[i][j]), qa[2 * j]); qa[2 * j + 1] = fmaf(hv, hi16(wqr[i][j]), qa[2 * j + 1]); } }
#pragma unroll
        for (int j = 0; j < 8; ++j) { qa[j] += __shfl_xor(qa[j], 16); qa[j] += __shfl_xor(qa[j], 32); }
        if (r == 0) {
#pragma unroll
            for (int j = 0; j < 8; ++j) QRED[wave * DH + 8 * c + j] = qa[j];
        }
    }
    __syncthreads();
    u32x4 qp;
    {
#pragma unroll
        for (int j = 0; j < 4; ++j) { float s0 = 0.f, s1 = 0.f;
#pragma unroll
            for (int w8 = 0; w8 < NW; ++w8) { s0 += QRED[w8 * DH + 8 * c + 2 * j]; s1 += QRED[w8 * DH + 8 * c + 2 * j + 1]; }
            qp[j] = f2bfu(s0) | (f2bfu(s1) << 16); }
    }

    float lmax = -INFINITY;
#pragma unroll 4
    for (int it = 0; it < NIT; ++it) {
        const int cur = it % DEPTH;
        float x[4];
#pragma unroll
        for (int t = 0; t < 4; ++t) { float d = 0.f;
#pragma unroll
            for (int j = 0; j < 4; ++j) d = dot2(kb[cur][t][j], qp[j], d);
            x[t] = d; }
        if (it + DEPTH < NIT) {
#pragma unroll
            for (int t = 0; t < 4; ++t) kb[cur][t] = ldnt(kp + (size_t)((it + DEPTH) * 16 + 4 * t) * DH);
        } else {
#pragma unroll
            for (int t = 0; t < 4; ++t) kb[cur][t] = ldnt(vp + (size_t)((it + DEPTH - NIT) * 16 + 4 * t) * DH);
        }
        const float y0 = ((c & 8) ? x[2] : x[0]) + dpp<XOR8>((c & 8) ? x[0] : x[2]);
        const float y1 = ((c & 8) ? x[3] : x[1]) + dpp<XOR8>((c & 8) ? x[1] : x[3]);
        float z = ((c & 4) ? y1 : y0) + dpp<XOR7>((c & 4) ? y0 : y1);
        z += dpp<XOR2>(z); z += dpp<XOR1>(z);
        const int ti = ((c >> 3) & 1) * 2 + ((c >> 2) & 1);
        const float sc = rbf(rbf(z) * 0.08838834764831845f);
        lmax = fmaxf(lmax, sc);
        if ((c & 3) == 0) S[wave * KPW + it * 16 + r * 4 + ti] = sc;
    }
#pragma unroll
    for (int o = 32; o > 0; o >>= 1) lmax = fmaxf(lmax, __shfl_xor(lmax, o));
    if (lane == 0) wmax[wave] = lmax;
    __syncthreads();
    float M = wmax[0];
#pragma unroll
    for (int w8 = 1; w8 < NW; ++w8) M = fmaxf(M, wmax[w8]);
    float ls;
    { float4* sp4 = reinterpret_cast<float4*>(S + wave * KPW) + lane; float4 v4 = *sp4;
      v4.x = __expf(v4.x - M); v4.y = __expf(v4.y - M); v4.z = __expf(v4.z - M); v4.w = __expf(v4.w - M);
      ls = (v4.x + v4.y) + (v4.z + v4.w); *sp4 = v4; }
#pragma unroll
    for (int o = 32; o > 0; o >>= 1) ls += __shfl_xor(ls, o);
    if (lane == 0) wsum[wave] = ls;

    float acc[8];
#pragma unroll
    for (int j = 0; j < 8; ++j) acc[j] = 0.f;
    const u16* wop = p.wo + (size_t)(h * DH + wave * 16) * DM + lane * 8;
#pragma unroll 4
    for (int it = 0; it < NIT; ++it) {
        const int cur = it % DEPTH;
        const float4 p4 = *reinterpret_cast<const float4*>(S + wave * KPW + it * 16 + r * 4);
        const float pp[4] = {p4.x, p4.y, p4.z, p4.w};
#pragma unroll
        for (int t = 0; t < 4; ++t) {
#pragma unroll
            for (int j = 0; j < 4; ++j) { acc[2 * j] = fmaf(pp[t], lo16(kb[cur][t][j]), acc[2 * j]); acc[2 * j + 1] = fmaf(pp[t], hi16(kb[cur][t][j]), acc[2 * j + 1]); }
        }
        if (it + DEPTH < NIT) {
#pragma unroll
            for (int t = 0; t < 4; ++t) kb[cur][t] = ldnt(vp + (size_t)((it + DEPTH) * 16 + 4 * t) * DH);
        } else {
#pragma unroll
            for (int t = 0; t < 4; ++t) kb[cur][t] = ld16(wop + (size_t)((it + DEPTH - NIT) * 4 + t) * DM);
        }
    }
    const bool do_gu = jq < 28, do_dn = jq < 8;
    const int gsl = xq * 28 + jq, dsl = xq * 8 + jq;
    u32x4 wgr = {0, 0, 0, 0}, wur = {0, 0, 0, 0}, wdr[4];
#pragma unroll
    for (int i = 0; i < 4; ++i) wdr[i] = u32x4{0, 0, 0, 0};
    if (do_gu) { wgr = ld16(p.wg + (size_t)tid * FF + gsl * 8); wur = ld16(p.wu + (size_t)tid * FF + gsl * 8); }
    if (do_dn) {
#pragma unroll
        for (int i = 0; i < 4; ++i) if (tid + 512 * i < FF) wdr[i] = ld16(p.wd + (size_t)(tid + 512 * i) * DM + dsl * 8);
    }
#pragma unroll
    for (int j = 0; j < 8; ++j) { acc[j] += __shfl_xor(acc[j], 16); acc[j] += __shfl_xor(acc[j], 32); }
    if (r == 0) {
#pragma unroll
        for (int j = 0; j < 8; ++j) YRED[wave * DH + 8 * c + j] = acc[j];
    }
    __syncthreads();
    if (tid < DH) { float s = 0.f;
#pragma unroll
        for (int w8 = 0; w8 < NW; ++w8) s += YRED[w8 * DH + tid];
        obuf[tid] = s; }
    __syncthreads();
    float L = 0.f;
#pragma unroll
    for (int w8 = 0; w8 < NW; ++w8) L += wsum[w8];
    {
        float ya[8];
#pragma unroll
        for (int j = 0; j < 8; ++j) ya[j] = 0.f;
#pragma unroll
        for (int i = 0; i < 16; ++i) { const float ov = obuf[wave * 16 + i];
#pragma unroll
            for (int j = 0; j < 4; ++j) { ya[2 * j] = fmaf(ov, lo16(kb[i >> 2][i & 3][j]), ya[2 * j]); ya[2 * j + 1] = fmaf(ov, hi16(kb[i >> 2][i & 3][j]), ya[2 * j + 1]); } }
        __syncthreads();
        *reinterpret_cast<float4*>(YRED + wave * DM + lane * 8) = make_float4(ya[0], ya[1], ya[2], ya[3]);
        *reinterpret_cast<float4*>(YRED + wave * DM + lane * 8 + 4) = make_float4(ya[4], ya[5], ya[6], ya[7]);
    }
    __syncthreads();
    u64* GY = reinterpret_cast<u64*>(p.ws + WS_GY);
    u64* GML = reinterpret_cast<u64*>(p.ws + WS_GML);
    u64* GX1 = reinterpret_cast<u64*>(p.ws + WS_GX1);
    u64* GACT = reinterpret_cast<u64*>(p.ws + WS_GACT);
    unsigned* TMO = reinterpret_cast<unsigned*>(p.ws + WS_TMO);
    {
        float s = 0.f;
#pragma unroll
        for (int w8 = 0; w8 < NW; ++w8) s += YRED[w8 * DM + tid];
        const int pidx = h * 2 + sp;
        st_gran(GY + ((size_t)((b * 64 + (tid >> 3)) * 64 + pidx)) * 8 + (tid & 7), TAG_Y, __float_as_uint(s));
        if (tid < 2) st_gran(GML + (size_t)(b * 64 + pidx) * 2 + tid, TAG_Y, __float_as_uint(tid == 0 ? M : L));
    }
    const int b2 = bid & 3, sl2 = bid >> 2;
    float* X1 = HB;
    float* H2 = QRED;
    if (wave == 0) {
        u64 g[8], gm, gl; int spins = 0;
        const u64* gy = GY + ((size_t)((b2 * 64 + sl2) * 64 + lane)) * 8;
        const u64* gml = GML + (size_t)(b2 * 64 + lane) * 2;
        for (;;) {
            bool ok = true;
#pragma unroll
            for (int j = 0; j < 8; ++j) g[j] = ld_gran(gy + j);
            gm = ld_gran(gml); gl = ld_gran(gml + 1);
#pragma unroll
            for (int j = 0; j < 8; ++j) ok &= (unsigned)(g[j] >> 32) == TAG_Y;
            ok &= (unsigned)(gm >> 32) == TAG_Y; ok &= (unsigned)(gl >> 32) == TAG_Y;
            if (__all(ok)) break;
            if (++spins > SPIN_CAP) { if (lane == 0) atomicAdd(TMO, 1u); break; }
            __builtin_amdgcn_s_sleep(1);
        }
        const float m = __uint_as_float((unsigned)gm), l = __uint_as_float((unsigned)gl);
        const float mo = __shfl_xor(m, 1), lo = __shfl_xor(l, 1);
        const float Mh = fmaxf(m, mo), e = __expf(m - Mh), eo = __expf(mo - Mh);
        const float scl = e / (e * l + eo * lo);
        float a8[8];
#pragma unroll
        for (int j = 0; j < 8; ++j) { float t = __uint_as_float((unsigned)g[j]) * scl;
#pragma unroll
            for (int o = 32; o > 0; o >>= 1) t += __shfl_xor(t, o);
            a8[j] = t; }
        if (lane < 4) {
            float xv[2];
#pragma unroll
            for (int k = 0; k < 2; ++k) { const int col = sl2 * 8 + 2 * lane + k; const float at = rbf(lane == 0 ? a8[k] : lane == 1 ? a8[2 + k] : lane == 2 ? a8[4 + k] : a8[6 + k]);
                xv[k] = rbf(bf2f(p.x[b2 * DM + col]) + rbf(MOD[b2 * (6 * DM) + 2 * DM + col] * at)); }
            st_gran(GX1 + b2 * 256 + sl2 * 4 + lane, TAG_X1, f2bfu(xv[0]) | (f2bfu(xv[1]) << 16));
        }
    }
    {
        u64 g0, g1; int spins = 0;
        for (;;) {
            g0 = ld_gran(GX1 + tid); g1 = ld_gran(GX1 + 512 + tid);
            const bool ok = ((unsigned)(g0 >> 32) == TAG_X1) && ((unsigned)(g1 >> 32) == TAG_X1);
            if (__all(ok)) break;
            if (++spins > SPIN_CAP) { if (lane == 0) atomicAdd(TMO, 1u); break; }
            __builtin_amdgcn_s_sleep(1);
        }
        *reinterpret_cast<float2*>(X1 + 2 * tid) = make_float2(lo16((unsigned)g0), hi16((unsigned)g0));
        *reinterpret_cast<float2*>(X1 + 1024 + 2 * tid) = make_float2(lo16((unsigned)g1), hi16((unsigned)g1));
    }
    __syncthreads();
    if (wave < 4) {
        const int bb = wave; float v[8]; float s = 0.f;
#pragma unroll
        for (int j = 0; j < 8; ++j) { v[j] = X1[bb * DM + lane * 8 + j]; s += v[j]; }
#pragma unroll
        for (int o = 32; o > 0; o >>= 1) s += __shfl_xor(s, o);
        const float mean = s * (1.f / DM);
        float q2 = 0.f;
#pragma unroll
        for (int j = 0; j < 8; ++j) { const float d = v[j] - mean; q2 += d * d; }
#pragma unroll
        for (int o = 32; o > 0; o >>= 1) q2 += __shfl_xor(q2, o);
        const float var = rbf(q2 * (1.f / DM)), mb = rbf(mean);
        const float den = rbf(sqrtf(rbf(var + 1e-5f)));
#pragma unroll
        for (int j = 0; j < 8; ++j) { const int i = lane * 8 + j;
            H2[bb * DM + i] = rbf(rbf(rbf(rbf(v[j] - mb) / den) * rbf(1.f + MOD[bb * (6 * DM) + 4 * DM + i])) + MOD[bb * (6 * DM) + 3 * DM + i]); }
    }
    __syncthreads();
    if (do_gu) {
        float vg[32], vu[32];
#pragma unroll
        for (int bb = 0; bb < 4; ++bb) { const float hv = H2[bb * DM + tid];
#pragma unroll
            for (int j = 0; j < 4; ++j) { vg[bb * 8 + 2 * j] = hv * lo16(wgr[j]); vg[bb * 8 + 2 * j + 1] = hv * hi16(wgr[j]);
                                          vu[bb * 8 + 2 * j] = hv * lo16(wur[j]); vu[bb * 8 + 2 * j + 1] = hv * hi16(wur[j]); } }
        const float tg = treduce32(vg, lane), tu = treduce32(vu, lane);
        if (lane < 32) { red[wave * 64 + lane] = tg; red[wave * 64 + 32 + lane] = tu; }
    }
    __syncthreads();
    if (do_gu && tid < 32) {
        float g = 0.f, u = 0.f;
#pragma unroll
        for (int w8 = 0; w8 < NW; ++w8) { g += red[w8 * 64 + tid]; u += red[w8 * 64 + 32 + tid]; }
        g = rbf(g); u = rbf(u);
        const float inner = 0.7978845608028654f * (g + 0.044715f * g * g * g);
        const float gl = rbf(0.5f * g * (1.f + tanhf(inner)));
        const float a = rbf(gl * u);
        const float an = __shfl_down(a, 1);
        if ((tid & 1) == 0) st_gran(GACT + (tid >> 3) * 896 + gsl * 4 + ((tid & 7) >> 1), TAG_ACT, f2bfu(a) | (f2bfu(an) << 16));
    }
    if (do_dn) {
        float* ACT = YRED;
        { u64 g[7]; int spins = 0;
          for (;;) { bool ok = true;
#pragma unroll
              for (int i = 0; i < 7; ++i) g[i] = ld_gran(GACT + tid + 512 * i);
#pragma unroll
              for (int i = 0; i < 7; ++i) ok &= (unsigned)(g[i] >> 32) == TAG_ACT;
              if (__all(ok)) break;
              if (++spins > SPIN_CAP) { if (lane == 0) atomicAdd(TMO, 1u); break; }
              __builtin_amdgcn_s_sleep(1); }
#pragma unroll
          for (int i = 0; i < 7; ++i) *reinterpret_cast<float2*>(ACT + 2 * (tid + 512 * i)) = make_float2(lo16((unsigned)g[i]), hi16((unsigned)g[i])); }
        __syncthreads();
        float v[32];
#pragma unroll
        for (int k = 0; k < 32; ++k) v[k] = 0.f;
#pragma unroll
        for (int i = 0; i < 4; ++i) { const int row = tid + 512 * i; if (row < FF) {
#pragma unroll
            for (int bb = 0; bb < 4; ++bb) { const float av = ACT[bb * FF + row];
#pragma unroll
                for (int j = 0; j < 4; ++j) { v[bb * 8 + 2 * j] = fmaf(av, lo16(wdr[i][j]), v[bb * 8 + 2 * j]); v[bb * 8 + 2 * j + 1] = fmaf(av, hi16(wdr[i][j]), v[bb * 8 + 2 * j + 1]); } } } }
        const float t = treduce32(v, lane);
        __syncthreads();
        if (lane < 32) red[wave * 32 + lane] = t;
        __syncthreads();
        if (tid < 32) { float s = 0.f;
#pragma unroll
            for (int w8 = 0; w8 < NW; ++w8) s += red[w8 * 32 + tid];
            const int bb = tid >> 3, col = dsl * 8 + (tid & 7);
            p.out[bb * DM + col] = (u16)f2bfu(X1[bb * DM + col] + rbf(MOD[bb * (6 * DM) + 5 * DM + col] * rbf(s))); }
    }
}

extern "C" void kernel_launch(void* const* d_in, const int* in_sizes, int n_in, void* d_out, int out_size, void* d_ws, size_t ws_size, hipStream_t stream) {
    Params p;
    p.x = (const u16*)d_in[0]; p.kc = (const u16*)d_in[1]; p.vc = (const u16*)d_in[2]; p.wq = (const u16*)d_in[3]; p.wo = (const u16*)d_in[4];
    p.wg = (const u16*)d_in[6]; p.wu = (const u16*)d_in[7]; p.wd = (const u16*)d_in[8]; p.c = (const u16*)d_in[9]; p.wmod = (const u16*)d_in[10];
    p.out = (u16*)d_out; p.ws = (unsigned char*)d_ws;
    k_mod<<<384, 512, 0, stream>>>(p);
    k_fused<<<256, 512, 0, stream>>>(p);
}
```

```cpp
#include <hip/hip_runtime.h>
#include <hip/hip_bf16.h>
#include <cstdint>

typedef unsigned short u16;
typedef unsigned long long u64;
typedef __attribute__((ext_vector_type(4))) unsigned u32x4;
typedef __attribute__((ext_vector_type(2))) __bf16 bf16x2;

#define NB 4
#define DM 512
#define NH 32
#define SK 4096
#define DH 128
#define FF 1792
#define KPS 2048
#define NW 8
#define KPW (KPS / NW)
#define NIT (KPW / 16)
#define DEPTH 4

#define WS_MOD 0
#define WS_GY 65536
#define WS_GML (WS_GY + 4 * 64 * 64 * 8 * 8)
#define WS_GX1 (WS_GML + 4 * 64 * 2 * 8)
#define WS_GACT (WS_GX1 + 4 * 256 * 8)
#define WS_TMO (WS_GACT + 4 * 896 * 8)
#define WS_END (WS_TMO + 64)
#define TAG_Y 0x71A50001u
#define TAG_X1 0x71A50002u
#define TAG_ACT 0x71A50003u
#define SPIN_CAP 400000

__device__ __forceinline__ float bf2f(u16 u) { return __uint_as_float(((unsigned)u) << 16); }
__device__ __forceinline__ unsigned f2bfu(float f) { unsigned u = __float_as_uint(f); u += 0x7fffu + ((u >> 16) & 1u); return u >> 16; }
__device__ __forceinline__ float rbf(float f) { return __uint_as_float(f2bfu(f) << 16); }
__device__ __forceinline__ float lo16(unsigned d) { return __uint_as_float(d << 16); }
__device__ __forceinline__ float hi16(unsigned d) { return __uint_as_float(d & 0xffff0000u); }
__device__ __forceinline__ float dot2(unsigned k, unsigned q, float acc) {
    return __builtin_amdgcn_fdot2_f32_bf16(__builtin_bit_cast(bf16x2, k), __builtin_bit_cast(bf16x2, q), acc, false);
}
template <int CTRL> __device__ __forceinline__ float dpp(float x) {
    return __builtin_bit_cast(float, __builtin_amdgcn_mov_dpp(__builtin_bit_cast(int, x), CTRL, 0xf, 0xf, true));
}
constexpr int XOR1 = 0xB1, XOR2 = 0x4E, XOR7 = 0x141, XOR8 = 0x128;
__device__ __forceinline__ float swap_add32(float a, float b) {
    auto r = __builtin_amdgcn_permlane32_swap(__float_as_uint(a), __float_as_uint(b), false, false);
    return __uint_as_float(r[0]) + __uint_as_float(r[1]);
}
__device__ __forceinline__ float swap_add16(float a, float b) {
    auto r = __builtin_amdgcn_permlane16_swap(__float_as_uint(a), __float_as_uint(b), false, false);
    return __uint_as_float(r[0]) + __uint_as_float(r[1]);
}
__device__ __forceinline__ u32x4 ldnt(const u16* p) { return __builtin_nontemporal_load(reinterpret_cast<const u32x4*>(p)); }
__device__ __forceinline__ u32x4 ld16(const u16* p) { return *reinterpret_cast<const u32x4*>(p); }
__device__ __forceinline__ void st_gran(u64* p, unsigned tag, unsigned v) { __hip_atomic_store(p, ((u64)tag << 32) | (u64)v, __ATOMIC_RELAXED, __HIP_MEMORY_SCOPE_AGENT); }
__device__ __forceinline__ u64 ld_gran(const u64* p) { return __hip_atomic_load(p, __ATOMIC_RELAXED, __HIP_MEMORY_SCOPE_AGENT); }

__device__ __forceinline__ float treduce32(float (&v)[32], int lane) {
#pragma unroll
    for (int i = 0; i < 32; ++i) v[i] = swap_add32(v[i], v[i]);
#pragma unroll
    for (int i = 0; i < 16; ++i) v[i] = swap_add16(v[i], v[i + 16]);
#pragma unroll
    for (int i = 0; i < 8; ++i) { const bool up = lane & 8; const float keep = up ? v[i + 8] : v[i], send = up ? v[i] : v[i + 8]; v[i] = keep + dpp<XOR8>(send); }
#pragma unroll
    for (int i = 0; i < 4; ++i) { const bool up = lane & 4; const float keep = up ? v[i + 4] : v[i], send = up ? v[i] : v[i + 4]; v[i] = keep + dpp<XOR7>(send); }
#pragma unroll
    for (int i = 0; i < 2; ++i) { const bool up = lane & 2; const float keep = up ? v[i + 2] : v[i], send = up ? v[i] : v[i + 2]; v[i] = keep + dpp<XOR2>(send); }
    { const bool up = lane & 1; const float keep = up ? v[1] : v[0], send = up ? v[0] : v[1]; v[0] = keep + dpp<XOR1>(send); }
    return v[0];
}

struct Params { const u16 *x, *kc, *vc, *wq, *wo, *wg, *wu, *wd, *c, *wmod; u16* out; unsigned char* ws; };

__global__ void __launch_bounds__(512) k_mod(Params p) {
    __shared__ float red[NW * 32];
    const int tid = threadIdx.x, lane = tid & 63, wave = tid >> 6;
    const int sl = (blockIdx.x & 7) * 48 + (blockIdx.x >> 3);
    const u32x4 w = ld16(p.wmod + (size_t)tid * (6 * DM) + sl * 8);
    float a[4];
#pragma unroll
    for (int b = 0; b < 4; ++b) { const float v = bf2f(p.c[b * DM + tid]); a[b] = rbf(v / (1.f + __expf(-v))); }
    { u32x4* z = reinterpret_cast<u32x4*>(p.ws + WS_GY); const int n = (WS_END - WS_GY) / 16;
      for (int i = blockIdx.x * 512 + tid; i < n; i += gridDim.x * 512) z[i] = u32x4{0u, 0u, 0u, 0u}; }
    float v[32];
#pragma unroll
    for (int b = 0; b < 4; ++b)
#pragma unroll
        for (int j = 0; j < 4; ++j) { v[b * 8 + 2 * j] = a[b] * lo16(w[j]); v[b * 8 + 2 * j + 1] = a[b] * hi16(w[j]); }
    const float t = treduce32(v, lane);
    if (lane < 32) red[wave * 32 + lane] = t;
    __syncthreads();
    if (tid < 32) { float s = 0.f;
#pragma unroll
        for (int w8 = 0; w8 < NW; ++w8) s += red[w8 * 32 + tid];
        reinterpret_cast<float*>(p.ws + WS_MOD)[(tid >> 3) * (6 * DM) + sl * 8 + (tid & 7)] = rbf(s); }
}

__global__ void __launch_bounds__(512, 2) k_fused(Params p) {
    __shared__ __attribute__((aligned(16))) float lds[4096 + 2048 + 7168 + 1024];
    float* HB = lds;
    float* QRED = lds + 2048;
    float* S = lds + 4096;
    float* YRED = lds + 6144;
    float* SM = lds + 6144 + 7168;
    float* wmax = SM, *wsum = SM + 8, *obuf = SM + 16, *ored = SM + 144, *red = SM + 160;
    const float* MOD = reinterpret_cast<const float*>(p.ws + WS_MOD);
    const int bid = blockIdx.x, xq = bid & 7, jq = bid >> 3;
    const int h = xq * 4 + (jq >> 3), b = (jq & 7) >> 1, sp = jq & 1;
    const int tid = threadIdx.x, lane = tid & 63, wave = __builtin_amdgcn_readfirstlane(tid >> 6), r = lane >> 4, c = lane & 15;

    u32x4 xr = {0, 0, 0, 0}; float sa[8], ca[8];
    if (wave == 0) {
        xr = ld16(p.x + b * DM + lane * 8);
#pragma unroll
        for (int j = 0; j < 8; ++j) { sa[j] = MOD[b * (6 * DM) + lane * 8 + j]; ca[j] = MOD[b * (6 * DM) + DM + lane * 8 + j]; }
    }
    u32x4 wqr[16];
    { const u16* wp = p.wq + (size_t)(wave * 64 + r) * (NH * DH) + h * DH + 8 * c;
#pragma unroll
      for (int i = 0; i < 16; ++i) wqr[i] = ld16(wp + (size_t)(4 * i) * (NH * DH)); }
    const size_t kvbase = ((size_t)(b * NH + h) * SK + (size_t)sp * KPS + (size_t)wave * KPW) * DH;
    const u16* kp = p.kc + kvbase + (size_t)r * DH + 8 * c;
    const u16* vp = p.vc + kvbase + (size_t)r * DH + 8 * c;
    u32x4 kb[DEPTH][4];
#pragma unroll
    for (int d = 0; d < DEPTH; ++d) {
        __builtin_amdgcn_sched_barrier(0);
#pragma unroll
        for (int t = 0; t < 4; ++t) kb[d][t] = ldnt(kp + (size_t)(d * 16 + 4 * t) * DH);
    }
    __builtin_amdgcn_sched_barrier(0);

    if (wave == 0) {
        float v[8]; float s = 0.f;
#pragma unroll
        for (int j = 0; j < 4; ++j) { v[2 * j] = lo16(xr[j]); v[2 * j + 1] = hi16(xr[j]); }
#pragma unroll
        for (int j = 0; j < 8; ++j) s += v[j];
#pragma unroll
        for (int o = 32; o > 0; o >>= 1) s += __shfl_xor(s, o);
        const float mean = s * (1.f / DM);
        float q2 = 0.f;
#pragma unroll
        for (int j = 0; j < 8; ++j) { const float d = v[j] - mean; q2 += d * d; }
#pragma unroll
        for (int o = 32; o > 0; o >>= 1) q2 += __shfl_xor(q2, o);
        const float var = rbf(q2 * (1.f / DM)), mb = rbf(mean);
        const float den = rbf(sqrtf(rbf(var + 1e-5f)));
#pragma unroll
        for (int j = 0; j < 8; ++j) HB[lane * 8 + j] = rbf(rbf(rbf(rbf(v[j] - mb) / den) * rbf(1.f + ca[j])) + sa[j]);
    }
    __syncthreads();
    {
        float qa[8];
#pragma unroll
        for (int j = 0; j < 8; ++j) qa[j] = 0.f;
#pragma unroll
        for (int i = 0; i < 16; ++i) { const float hv = HB[wave * 64 + 4 * i + r];
#pragma unroll
            for (int j = 0; j < 4; ++j) { qa[2 * j] = fmaf(hv, lo16(wqr[i][j]), qa[2 * j]); qa[2 * j + 1] = fmaf(hv, hi16(wqr[i][j]), qa[2 * j + 1]); } }
#pragma unroll
        for (int j = 0; j < 8; ++j) { qa[j] += __shfl_xor(qa[j], 16); qa[j] += __shfl_xor(qa[j], 32); }
        if (r == 0) {
#pragma unroll
            for (int j = 0; j < 8; ++j) QRED[wave * DH + 8 * c + j] = qa[j];
        }
    }
    __syncthreads();
    u32x4 qp;
    {
#pragma unroll
        for (int j = 0; j < 4; ++j) { float s0 = 0.f, s1 = 0.f;
#pragma unroll
            for (int w8 = 0; w8 < NW; ++w8) { s0 += QRED[w8 * DH + 8 * c + 2 * j]; s1 += QRED[w8 * DH + 8 * c + 2 * j + 1]; }
            qp[j] = f2bfu(s0) | (f2bfu(s1) << 16); }
    }

    float lmax = -INFINITY;
#define KBODY(it, cur, RELOAD) { \
        float x[4]; \
        _Pragma("unroll") for (int t = 0; t < 4; ++t) { float dd = 0.f; \
            _Pragma("unroll") for (int j = 0; j < 4; ++j) dd = dot2(kb[cur][t][j], qp[j], dd); \
            x[t] = dd; } \
        _Pragma("unroll") for (int t = 0; t < 4; ++t) kb[cur][t] = RELOAD; \
        const float y0 = ((c & 8) ? x[2] : x[0]) + dpp<XOR8>((c & 8) ? x[0] : x[2]); \
        const float y1 = ((c & 8) ? x[3] : x[1]) + dpp<XOR8>((c & 8) ? x[1] : x[3]); \
        float z = ((c & 4) ? y1 : y0) + dpp<XOR7>((c & 4) ? y0 : y1); \
        z += dpp<XOR2>(z); z += dpp<XOR1>(z); \
        const int ti = ((c >> 3) & 1) * 2 + ((c >> 2) & 1); \
        const float sc = rbf(rbf(z) * 0.08838834764831845f); \
        lmax = fmaxf(lmax, sc); \
        if ((c & 3) == 0) S[wave * KPW + (it) * 16 + r * 4 + ti] = sc; \
        __builtin_amdgcn_sched_barrier(0); }
#pragma unroll 1
    for (int i4 = 0; i4 < NIT - DEPTH; i4 += DEPTH) {
#pragma unroll
        for (int d = 0; d < DEPTH; ++d) KBODY(i4 + d, d, ldnt(kp + (size_t)((i4 + d + DEPTH) * 16 + 4 * t) * DH))
    }
#pragma unroll
    for (int d = 0; d < DEPTH; ++d) KBODY(NIT - DEPTH + d, d, ldnt(vp + (size_t)(d * 16 + 4 * t) * DH))
#pragma unroll
    for (int o = 32; o > 0; o >>= 1) lmax = fmaxf(lmax, __shfl_xor(lmax, o));
    if (lane == 0) wmax[wave] = lmax;
    __syncthreads();
    float M = wmax[0];
#pragma unroll
    for (int w8 = 1; w8 < NW; ++w8) M = fmaxf(M, wmax[w8]);
    float ls;
    { float4* sp4 = reinterpret_cast<float4*>(S + wave * KPW) + lane; float4 v4 = *sp4;
      v4.x = __expf(v4.x - M); v4.y = __expf(v4.y - M); v4.z = __expf(v4.z - M); v4.w = __expf(v4.w - M);
      ls = (v4.x + v4.y) + (v4.z + v4.w); *sp4 = v4; }
#pragma unroll
    for (int o = 32; o > 0; o >>= 1) ls += __shfl_xor(ls, o);
    if (lane == 0) wsum[wave] = ls;

    float acc[8];
#pragma unroll
    for (int j = 0; j < 8; ++j) acc[j] = 0.f;
    const u16* wop = p.wo + (size_t)(h * DH + wave * 16) * DM + lane * 8;
#define VBODY(it, cur, RELOAD) { \
        const float4 p4 = *reinterpret_cast<const float4*>(S + wave * KPW + (it) * 16 + r * 4); \
        const float pp[4] = {p4.x, p4.y, p4.z, p4.w}; \
        _Pragma("unroll") for (int t = 0; t < 4; ++t) { \
            _Pragma("unroll") for (int j = 0; j < 4; ++j) { acc[2 * j] = fmaf(pp[t], lo16(kb[cur][t][j]), acc[2 * j]); acc[2 * j + 1] = fmaf(pp[t], hi16(kb[cur][t][j]), acc[2 * j + 1]); } \
        } \
        _Pragma("unroll") for (int t = 0; t < 4; ++t) kb[cur][t] = RELOAD; \
        __builtin_amdgcn_sched_barrier(0); }
#pragma unroll 1
    for (int i4 = 0; i4 < NIT - DEPTH; i4 += DEPTH) {
#pragma unroll
        for (int d = 0; d < DEPTH; ++d) VBODY(i4 + d, d, ldnt(vp + (size_t)((i4 + d + DEPTH) * 16 + 4 * t) * DH))
    }
#pragma unroll
    for (int d = 0; d < DEPTH; ++d) VBODY(NIT - DEPTH + d, d, ld16(wop + (size_t)(d * 4 + t) * DM))
    const bool do_gu = jq < 28, do_dn = jq < 8;
    const int gsl = xq * 28 + jq, dsl = xq * 8 + jq;
    u32x4 wgr = {0, 0, 0, 0}, wur = {0, 0, 0, 0}, wdr[4];
#pragma unroll
    for (int i = 0; i < 4; ++i) wdr[i] = u32x4{0, 0, 0, 0};
    if (do_gu) { wgr = ld16(p.wg + (size_t)tid * FF + gsl * 8); wur = ld16(p.wu + (size_t)tid * FF + gsl * 8); }
    if (do_dn) {
#pragma unroll
        for (int i = 0; i < 4; ++i) if (tid + 512 * i < FF) wdr[i] = ld16(p.wd + (size_t)(tid + 512 * i) * DM + dsl * 8);
    }
#pragma unroll
    for (int j = 0; j < 8; ++j) { acc[j] += __shfl_xor(acc[j], 16); acc[j] += __shfl_xor(acc[j], 32); }
    if (r == 0) {
#pragma unroll
        for (int j = 0; j < 8; ++j) YRED[wave * DH + 8 * c + j] = acc[j];
    }
    __syncthreads();
    if (tid < DH) { float s = 0.f;
#pragma unroll
        for (int w8 = 0; w8 < NW; ++w8) s += YRED[w8 * DH + tid];
        obuf[tid] = s; }
    __syncthreads();
    float L = 0.f;
#pragma unroll
    for (int w8 = 0; w8 < NW; ++w8) L += wsum[w8];
    {
        float ya[8];
#pragma unroll
        for (int j = 0; j < 8; ++j) ya[j] = 0.f;
#pragma unroll
        for (int i = 0; i < 16; ++i) { const float ov = obuf[wave * 16 + i];
#pragma unroll
            for (int j = 0; j < 4; ++j) { ya[2 * j] = fmaf(ov, lo16(kb[i >> 2][i & 3][j]), ya[2 * j]); ya[2 * j + 1] = fmaf(ov, hi16(kb[i >> 2][i & 3][j]), ya[2 * j + 1]); } }
        __syncthreads();
        *reinterpret_cast<float4*>(YRED + wave * DM + lane * 8) = make_float4(ya[0], ya[1], ya[2], ya[3]);
        *reinterpret_cast<float4*>(YRED + wave * DM + lane * 8 + 4) = make_float4(ya[4], ya[5], ya[6], ya[7]);
    }
    __syncthreads();
    u64* GY = reinterpret_cast<u64*>(p.ws + WS_GY);
    u64* GML = reinterpret_cast<u64*>(p.ws + WS_GML);
    u64* GX1 = reinterpret_cast<u64*>(p.ws + WS_GX1);
    u64* GACT = reinterpret_cast<u64*>(p.ws + WS_GACT);
    unsigned* TMO = reinterpret_cast<unsigned*>(p.ws + WS_TMO);
    {
        float s = 0.f;
#pragma unroll
        for (int w8 = 0; w8 < NW; ++w8) s += YRED[w8 * DM + tid];
        const int pidx = h * 2 + sp;
        st_gran(GY + ((size_t)((b * 64 + (tid >> 3)) * 64 + pidx)) * 8 + (tid & 7), TAG_Y, __float_as_uint(s));
        if (tid < 2) st_gran(GML + (size_t)(b * 64 + pidx) * 2 + tid, TAG_Y, __float_as_uint(tid == 0 ? M : L));
    }
    const int b2 = bid & 3, sl2 = bid >> 2;
    float* X1 = HB;
    float* H2 = QRED;
    if (wave == 0) {
        u64 gm, gl; int spins = 0;
        const u64* gml = GML + (size_t)b2 * 128;
        for (;;) {
            gm = ld_gran(gml + lane); gl = ld_gran(gml + 64 + lane);
            const bool ok = ((unsigned)(gm >> 32) == TAG_Y) && ((unsigned)(gl >> 32) == TAG_Y);
            if (__all(ok)) break;
            if (++spins > SPIN_CAP) { if (lane == 0) atomicAdd(TMO, 1u); break; }
            __builtin_amdgcn_s_sleep(4);
        }
        u64 g[8];
        const u64* gy = GY + ((size_t)((b2 * 64 + sl2) * 64 + lane)) * 8;
        spins = 0;
        for (;;) {
            bool ok = true;
#pragma unroll
            for (int j = 0; j < 8; ++j) g[j] = ld_gran(gy + j);
#pragma unroll
            for (int j = 0; j < 8; ++j) ok &= (unsigned)(g[j] >> 32) == TAG_Y;
            if (__all(ok)) break;
            if (++spins > SPIN_CAP) { if (lane == 0) atomicAdd(TMO, 1u); break; }
            __builtin_amdgcn_s_sleep(2);
        }
        const float fa = __uint_as_float((unsigned)gm), fb = __uint_as_float((unsigned)gl);
        const int s0 = (2 * lane) & 63;
        const float ma = __shfl(fa, s0), mb_ = __shfl(fb, s0), la = __shfl(fa, s0 + 1), lb_ = __shfl(fb, s0 + 1);
        const float m = lane < 32 ? ma : mb_, l = lane < 32 ? la : lb_;
        const float mo = __shfl_xor(m, 1), lo = __shfl_xor(l, 1);
        const float Mh = fmaxf(m, mo), e = __expf(m - Mh), eo = __expf(mo - Mh);
        const float scl = e / (e * l + eo * lo);
        float a8[8];
#pragma unroll
        for (int j = 0; j < 8; ++j) { float t = __uint_as_float((unsigned)g[j]) * scl;
#pragma unroll
            for (int o = 32; o > 0; o >>= 1) t += __shfl_xor(t, o);
            a8[j] = t; }
        if (lane < 4) {
            float xv[2];
#pragma unroll
            for (int k = 0; k < 2; ++k) { const int col = sl2 * 8 + 2 * lane + k; const float at = rbf(lane == 0 ? a8[k] : lane == 1 ? a8[2 + k] : lane == 2 ? a8[4 + k] : a8[6 + k]);
                xv[k] = rbf(bf2f(p.x[b2 * DM + col]) + rbf(MOD[b2 * (6 * DM) + 2 * DM + col] * at)); }
            st_gran(GX1 + b2 * 256 + sl2 * 4 + lane, TAG_X1, f2bfu(xv[0]) | (f2bfu(xv[1]) << 16));
        }
    }
    __syncthreads();
    {
        u64 g0, g1; int spins = 0;
        for (;;) {
            g0 = ld_gran(GX1 + tid); g1 = ld_gran(GX1 + 512 + tid);
            const bool ok = ((unsigned)(g0 >> 32) == TAG_X1) && ((unsigned)(g1 >> 32) == TAG_X1);
            if (__all(ok)) break;
            if (++spins > SPIN_CAP) { if (lane == 0) atomicAdd(TMO, 1u); break; }
            __builtin_amdgcn_s_sleep(2);
        }
        *reinterpret_cast<float2*>(X1 + 2 * tid) = make_float2(lo16((unsigned)g0), hi16((unsigned)g0));
        *reinterpret_cast<float2*>(X1 + 1024 + 2 * tid) = make_float2(lo16((unsigned)g1), hi16((unsigned)g1));
    }
    __syncthreads();
    if (wave < 4) {
        const int bb = wave; float v[8]; float s = 0.f;
#pragma unroll
        for (int j = 0; j < 8; ++j) { v[j] = X1[bb * DM + lane * 8 + j]; s += v[j]; }
#pragma unroll
        for (int o = 32; o > 0; o >>= 1) s += __shfl_xor(s, o);
        const float mean = s * (1.f / DM);
        float q2 = 0.f;
#pragma unroll
        for (int j = 0; j < 8; ++j) { const float d = v[j] - mean; q2 += d * d; }
#pragma unroll
        for (int o = 32; o > 0; o >>= 1) q2 += __shfl_xor(q2, o);
        const float var = rbf(q2 * (1.f / DM)), mb = rbf(mean);
        const float den = rbf(sqrtf(rbf(var + 1e-5f)));
#pragma unroll
        for (int j = 0; j < 8; ++j) { const int i = lane * 8 + j;
            H2[bb * DM + i] = rbf(rbf(rbf(rbf(v[j] - mb) / den) * rbf(1.f + MOD[bb * (6 * DM) + 4 * DM + i])) + MOD[bb * (6 * DM) + 3 * DM + i]); }
    }
    __syncthreads();
    if (do_gu) {
        float vg[32], vu[32];
#pragma unroll
        for (int bb = 0; bb < 4; ++bb) { const float hv = H2[bb * DM + tid];
#pragma unroll
            for (int j = 0; j < 4; ++j) { vg[bb * 8 + 2 * j] = hv * lo16(wgr[j]); vg[bb * 8 + 2 * j + 1] = hv * hi16(wgr[j]);
                                          vu[bb * 8 + 2 * j] = hv * lo16(wur[j]); vu[bb * 8 + 2 * j + 1] = hv * hi16(wur[j]); } }
        const float tg = treduce32(vg, lane), tu = treduce32(vu, lane);
        if (lane < 32) { red[wave * 64 + lane] = tg; red[wave * 64 + 32 + lane] = tu; }
    }
    __syncthreads();
    if (do_gu && tid < 32) {
        float g = 0.f, u = 0.f;
#pragma unroll
        for (int w8 = 0; w8 < NW; ++w8) { g += red[w8 * 64 + tid]; u += red[w8 * 64 + 32 + tid]; }
        g = rbf(g); u = rbf(u);
        const float inner = 0.7978845608028654f * (g + 0.044715f * g * g * g);
        const float gl = rbf(0.5f * g * (1.f + tanhf(inner)));
        const float a = rbf(gl * u);
        const float an = __shfl_down(a, 1);
        if ((tid & 1) == 0) st_gran(GACT + (tid >> 3) * 896 + gsl * 4 + ((tid & 7) >> 1), TAG_ACT, f2bfu(a) | (f2bfu(an) << 16));
    }
    if (do_dn) {
        float* ACT = YRED;
        { u64 g[7]; int spins = 0;
          for (;;) { bool ok = true;
#pragma unroll
              for (int i = 0; i < 7; ++i) g[i] = ld_gran(GACT + tid + 512 * i);
#pragma unroll
              for (int i = 0; i < 7; ++i) ok &= (unsigned)(g[i] >> 32) == TAG_ACT;
              if (__all(ok)) break;
              if (++spins > SPIN_CAP) { if (lane == 0) atomicAdd(TMO, 1u); break; }
              __builtin_amdgcn_s_sleep(2); }
#pragma unroll
          for (int i = 0; i < 7; ++i) *reinterpret_cast<float2*>(ACT + 2 * (tid + 512 * i)) = make_float2(lo16((unsigned)g[i]), hi16((unsigned)g[i])); }
        __syncthreads();
        float v[32];
#pragma unroll
        for (int k = 0; k < 32; ++k) v[k] = 0.f;
#pragma unroll
        for (int i = 0; i < 4; ++i) { const int row = tid + 512 * i; if (row < FF) {
#pragma unroll
            for (int bb = 0; bb < 4; ++bb) { const float av = ACT[bb * FF + row];
#pragma unroll
                for (int j = 0; j < 4; ++j) { v[bb * 8 + 2 * j] = fmaf(av, lo16(wdr[i][j]), v[bb * 8 + 2 * j]); v[bb * 8 + 2 * j + 1] = fmaf(av, hi16(wdr[i][j]), v[bb * 8 + 2 * j + 1]); } } } }
        const float t = treduce32(v, lane);
        __syncthreads();
        if (lane < 32) red[wave * 32 + lane] = t;
        __syncthreads();
        if (tid < 32) { float s = 0.f;
#pragma unroll
            for (int w8 = 0; w8 < NW; ++w8) s += red[w8 * 32 + tid];
            const int bb = tid >> 3, col = dsl * 8 + (tid & 7);
            p.out[bb * DM + col] = (u16)f2bfu(X1[bb * DM + col] + rbf(MOD[bb * (6 * DM) + 5 * DM + col] * rbf(s))); }
    }
}

extern "C" void kernel_launch(void* const* d_in, const int* in_sizes, int n_in, void* d_out, int out_size, void* d_ws, size_t ws_size, hipStream_t stream) {
    Params p;
    p.x = (const u16*)d_in[0]; p.kc = (const u16*)d_in[1]; p.vc = (const u16*)d_in[2]; p.wq = (const u16*)d_in[3]; p.wo = (const u16*)d_in[4];
    p.wg = (const u16*)d_in[6]; p.wu = (const u16*)d_in[7]; p.wd = (const u16*)d_in[8]; p.c = (const u16*)d_in[9]; p.wmod = (const u16*)d_in[10];
    p.out = (u16*)d_out; p.ws = (unsigned char*)d_ws;
    k_mod<<<384, 512, 0, stream>>>(p);
    k_fused<<<256, 512, 0, stream>>>(p);
}
```
